```python
import torch

def _softmax(x, axis=-1):
    return torch.softmax(x, dim=axis)

def _gelu(x):
    return torch.nn.functional.gelu(x, approximate='tanh')

def _silu(x):
    return torch.nn.functional.silu(x)

def _sigmoid(x):
    return torch.sigmoid(x)

def _layernorm(x, axis=-1, eps=1e-05):
    m = x.mean(dim=axis, keepdim=True)
    v = x.var(dim=axis, keepdim=True, unbiased=False)
    return (x - m) / torch.sqrt(v + eps)

def _scatter_add(tgt, idx, src, axis=0):
    return tgt.clone().index_add_(axis, idx, src)

def _segment_sum(src, seg, n):
    out = torch.zeros((n,) + src.shape[1:], dtype=src.dtype)
    return out.index_add_(0, seg, src)

def _pad(x, pw):
    flat = tuple((v for pair in reversed(pw) for v in pair))
    return torch.nn.functional.pad(x, flat)
kernel_name = 'standalone__tree_speculative_verify_tree_attention_over_candidate_token_tree__decode'

def setup_inputs():

    _w = lambda a: (lambda t: t.to(torch.bfloat16) if t.is_floating_point() else t)(torch.as_tensor(a))
    B, Hq, Hkv, T, C, D = (2, 32, 16, 1, 8192, 128)
    tree_mask = torch.zeros((B, T, T), dtype=float)
    for b in range(B):
        parent = torch.zeros(T, dtype=torch.int64)
        for i in range(1, T):
            parent[i] = int(torch.randint(0, i, (1,)).item())
        for i in range(T):
            j = i
            tree_mask[b, i, j] = 1.0
            while j > 0:
                j = parent[j]
                tree_mask[b, i, j] = 1.0
    return {'q': _w((lambda *s: torch.randn(*s, dtype=torch.bfloat16) * 0.8)(B, Hq, T, D)), 'k_cache': _w((lambda *s: torch.randn(*s, dtype=torch.bfloat16) * 0.8)(B, Hkv, C, D)), 'v_cache': _w((lambda *s: torch.randn(*s, dtype=torch.bfloat16) * 0.05)(B, Hkv, C, D)), 'k_tree': _w((lambda *s: torch.randn(*s, dtype=torch.bfloat16) * 0.8)(B, Hkv, T, D)), 'v_tree': _w((lambda *s: torch.randn(*s, dtype=torch.bfloat16) * 0.05)(B, Hkv, T, D)), 'tree_mask': _w(tree_mask)}

def reference(q, k_cache, v_cache, k_tree, v_tree, tree_mask):
    B, Hq, T, D = q.shape
    Hkv, C = (k_cache.shape[1], k_cache.shape[2])
    rep = Hq // Hkv
    kc = (lambda x, n, axis: torch.repeat_interleave(x, n, dim=axis))(k_cache, rep, axis=1)
    vc = (lambda x, n, axis: torch.repeat_interleave(x, n, dim=axis))(v_cache, rep, axis=1)
    kt = (lambda x, n, axis: torch.repeat_interleave(x, n, dim=axis))(k_tree, rep, axis=1)
    vt = (lambda x, n, axis: torch.repeat_interleave(x, n, dim=axis))(v_tree, rep, axis=1)
    k = torch.concatenate([kc, kt], axis=2)
    v = torch.concatenate([vc, vt], axis=2)
    scale = D ** (-0.5)
    scores = torch.einsum('bhqd,bhkd->bhqk', q, k) * scale
    ctx_mask = torch.ones((B, 1, T, C), dtype=scores.dtype)
    tm = tree_mask[:, None, :, :].clone()
    full_mask = torch.concatenate([ctx_mask, tm], axis=-1)
    scores = scores + (1.0 - full_mask) * -1000000000.0
    p = _softmax(scores, axis=-1)
    return torch.einsum('bhqk,bhkd->bhqd', p, v)
```

```cpp
#define ALIGN 1
#define AOFS -12
#define TLOOK 113
#include <hip/hip_runtime.h>
#include <hip/hip_bf16.h>
#include <stdint.h>

#define WLO 2
#define WHI 5
#define TGOAL 1715
#define TOFS (-25)
#define ENDSPIN 6
#define ENDOFS 40
#define CONS 0
#define SN 8
#define GST 14
#define FDLY 0
#define TWAVE 7
#define ORGE 0
#define WARM 0
#define WSTEP 17
#define LAYB 1
#define MIDOFS 10
#define STAG 0
#define RECX 0
#define ROT 0
#define STAMP 0
#define RT(x) do { __builtin_amdgcn_sched_barrier(0); asm volatile("s_memrealtime %0\n\ts_waitcnt lgkmcnt(0)" : "=s"(x) :: "memory"); __builtin_amdgcn_sched_barrier(0); } while (0)

typedef unsigned u32x16 __attribute__((ext_vector_type(16)));
typedef unsigned u32x4 __attribute__((ext_vector_type(4)));
typedef unsigned u32x2 __attribute__((ext_vector_type(2)));
typedef float f32x4 __attribute__((ext_vector_type(4)));
typedef __bf16 bf16x8 __attribute__((ext_vector_type(8)));
typedef __bf16 bf16x2 __attribute__((ext_vector_type(2)));
typedef float f32x2 __attribute__((ext_vector_type(2)));
#define LAS __attribute__((address_space(3)))

constexpr int B_ = 2, HQ = 32, HKV = 16, CTX = 8192, D = 128;
constexpr int NPAIR = B_ * HKV;
constexpr int NSPLIT = 8, WAVES = 8;
constexpr int NTHR = WAVES * 64;
constexpr int NU = CTX / NSPLIT / WAVES / 4;
constexpr int W0 = 4;
constexpr float SCALE = 0.088388347648318440f;
constexpr float C2 = SCALE * 1.4426950408889634f;
constexpr int OOB = 0x7fffff00;
constexpr int NT_ = 2, SC1 = 16, SC01 = 17;

constexpr int HDR_BYTES = 512, HDR_TM = 128, HDR_ML = 256;
constexpr int REC_OFF = NPAIR * HDR_BYTES;
constexpr int REC_BYTES = 128;
constexpr int WS_BYTES = REC_OFF + 2 * NPAIR * WAVES * NSPLIT * REC_BYTES;
constexpr int STAMP_OFF = 2 << 20;
constexpr int LDS_OSTR = 1024 + 32, LDS_ML = WAVES * LDS_OSTR, LDS_BYTES = LDS_ML + WAVES * 16;

constexpr int unit_off(int j) { return ((j & 1) << 15) | (((j >> 1) & 1) << 13) | (((j >> 2) & 1) << 20) | (((j >> 3) & 1) << 18) | (((j >> 4) & 1) << 17); }
constexpr int BSTEP = 9, GSTEP = GST, TSTEP = SN + 4 > 12 ? SN + 4 : 12, ASTEP = NU - 2 * WHI - 2;
constexpr int extra1(int n, int w, int e, int cnt) { return (n - w <= e && e < n) ? cnt : 0; }
constexpr int extra(int n, int w) {
  return extra1(n, w, W0 - 1, 1) + extra1(n, w, SN, 4) + extra1(n, w, NU + BSTEP + WHI, 1) + extra1(n, w, NU + GSTEP + WHI, 1) + extra1(n, w, TSTEP + WHI, 1) + extra1(n, w, ASTEP + WHI, 1) +
         extra1(n, w, NU + GSTEP + 2 * WHI + 1, 1) + extra1(n, w, NU + WSTEP + WHI, WARM ? 2 : 0);
}
constexpr int due(int n) { return TOFS + (n - 1) * TGOAL / 64; }

__device__ __forceinline__ f32x4 mfma16(u32x4 a, u32x4 b, f32x4 c) {
  return __builtin_amdgcn_mfma_f32_16x16x32_bf16(__builtin_bit_cast(bf16x8, a), __builtin_bit_cast(bf16x8, b), c, 0, 0, 0);
}
template <int CTRL> __device__ __forceinline__ float dpp(float x) {
  return __builtin_bit_cast(float, __builtin_amdgcn_mov_dpp(__builtin_bit_cast(int, x), CTRL, 0xf, 0xf, true));
}
constexpr int XOR1 = 0xB1, XOR2 = 0x4E, MIR8 = 0x141;
__device__ __forceinline__ float quad_diag_sum(f32x4 d) {
  return ((dpp<0x00>(d[0]) + dpp<0x55>(d[1])) + dpp<0xAA>(d[2])) + dpp<0xFF>(d[3]);
}
__device__ __forceinline__ float xg_max(float x) {
  auto s = __builtin_amdgcn_permlane16_swap(__float_as_uint(x), __float_as_uint(x), false, false);
  x = fmaxf(__uint_as_float(s[0]), __uint_as_float(s[1]));
  auto t = __builtin_amdgcn_permlane32_swap(__float_as_uint(x), __float_as_uint(x), false, false);
  return fmaxf(__uint_as_float(t[0]), __uint_as_float(t[1]));
}
__device__ __forceinline__ float xg_sum(float x) {
  auto s = __builtin_amdgcn_permlane16_swap(__float_as_uint(x), __float_as_uint(x), false, false);
  x = __uint_as_float(s[0]) + __uint_as_float(s[1]);
  auto t = __builtin_amdgcn_permlane32_swap(__float_as_uint(x), __float_as_uint(x), false, false);
  return __uint_as_float(t[0]) + __uint_as_float(t[1]);
}
__device__ __forceinline__ f32x4 sum8(f32x4 o, float f) {
  float a = o[0], b = o[1], c = o[2], d = o[3];
#define SUM8_STEP(ctrl)                                                           \
  "v_add_f32_dpp %0, %0, %0 " ctrl " row_mask:0xf bank_mask:0xf bound_ctrl:1\n\t" \
  "v_add_f32_dpp %1, %1, %1 " ctrl " row_mask:0xf bank_mask:0xf bound_ctrl:1\n\t" \
  "v_add_f32_dpp %2, %2, %2 " ctrl " row_mask:0xf bank_mask:0xf bound_ctrl:1\n\t" \
  "v_add_f32_dpp %3, %3, %3 " ctrl " row_mask:0xf bank_mask:0xf bound_ctrl:1\n\t"
  asm volatile("v_mul_f32 %0, %4, %0\n\tv_mul_f32 %1, %4, %1\n\tv_mul_f32 %2, %4, %2\n\tv_mul_f32 %3, %4, %3\n\t"
               SUM8_STEP("quad_perm:[1,0,3,2]") SUM8_STEP("quad_perm:[2,3,0,1]") SUM8_STEP("row_half_mirror")
               : "+v"(a), "+v"(b), "+v"(c), "+v"(d) : "v"(f));
  return f32x4{a, b, c, d};
}
__device__ __forceinline__ float reduce_scatter4(f32x4 o, float f) {
#pragma clang fp contract(off)
  o = o * f;
  auto a = __builtin_amdgcn_permlane16_swap(__float_as_uint(o[0]), __float_as_uint(o[1]), false, false);
  auto b = __builtin_amdgcn_permlane16_swap(__float_as_uint(o[2]), __float_as_uint(o[3]), false, false);
  const float u = __uint_as_float(a[0]) + __uint_as_float(a[1]);
  const float v = __uint_as_float(b[0]) + __uint_as_float(b[1]);
  auto c = __builtin_amdgcn_permlane32_swap(__float_as_uint(u), __float_as_uint(v), false, false);
  const float w = __uint_as_float(c[0]) + __uint_as_float(c[1]);
  return w + dpp<0x128>(w);
}
__device__ __forceinline__ float lane_pull(int byte_idx, float x) {
  return __builtin_bit_cast(float, __builtin_amdgcn_ds_bpermute(byte_idx, __builtin_bit_cast(int, x)));
}
__device__ __forceinline__ float merge_ml(float m, float l, float& M, float& L) {
#pragma clang fp contract(off)
  M = fmaxf(m, dpp<XOR1>(m));
  M = fmaxf(M, dpp<XOR2>(M));
  M = fmaxf(M, dpp<MIR8>(M));
  const float f = __builtin_amdgcn_exp2f(m - M);
  L = l * f;
  L = L + dpp<XOR1>(L);
  L = L + dpp<XOR2>(L);
  L = L + dpp<MIR8>(L);
  return f;
}
__device__ __forceinline__ unsigned pack_bf16(float lo, float hi) {
  f32x2 f = {lo, hi};
  bf16x2 b = __builtin_convertvector(f, bf16x2);
  return __builtin_bit_cast(unsigned, b);
}
template <typename T> __device__ __forceinline__ LAS T* lds_at(unsigned byte_addr) { return reinterpret_cast<LAS T*>(byte_addr); }

__attribute__((amdgpu_waves_per_eu(2, 2)))
__global__ void __launch_bounds__(NTHR) attn_tree_decode(const unsigned short* k_, const unsigned short* q_, const unsigned short* v_,
                                                         unsigned char* ws_, const unsigned short* kt_, const unsigned short* vt_,
                                                         unsigned short* out_, const unsigned short* mask_) {
  __shared__ __attribute__((aligned(16))) unsigned char lds[LDS_BYTES];
  u32x4 U[2 * NU];
  u32x4 Q;
  unsigned long long org;
  u32x16 ka;
  unsigned kw, qw, pf, a1, a2, a3;
  const unsigned wg = blockIdx.x;
  asm volatile(
      "s_load_dwordx16 %[ka], %[kp], 0x0\n\t"
      "s_getpc_b64 s[90:91]\n\t"
      "v_lshlrev_b32 %[pf], 4, %[tid]\n\t"
      "global_load_dword %[pf], %[pf], s[90:91] offset:256\n\t"
      "s_lshl_b32 s90, %[wg], 1\n\t"
      "v_lshlrev_b32 %[kw], 6, %[tid]\n\t"
      "s_xor_b32 s90, s90, %[wg]\n\t"
      "v_and_b32 %[a1], 0x180, %[tid]\n\t"
      "s_lshr_b32 s92, %[wg], 3\n\t"
      "v_and_b32 %[kw], 0x13c0, %[kw]\n\t"
      "s_bfe_u32 s90, s90, 0x10002\n\t"
      "v_mul_u32_u24 %[a1], 0x110, %[a1]\n\t"
      "s_lshl2_add_u32 s93, s92, s90\n\t"
      "v_and_or_b32 %[kw], %[tid], 48, %[kw]\n\t"
      "s_bfe_u32 s90, %[wg], 0x10001\n\t"
      "v_and_b32 %[a1], 0x10800, %[a1]\n\t"
      "s_lshl_b32 s93, s93, 5\n\t"
      "v_lshlrev_b32 %[qw], 5, %[tid]\n\t"
      "s_or_b32 s93, s93, s90\n\t"
      "v_and_b32 %[a2], 0xff, %[kw]\n\t"
      "s_and_b32 s90, %[wg], 1\n\t"
      "v_and_b32 %[qw], 0x100, %[qw]\n\t"
      "s_lshl4_add_u32 s93, s93, s90\n\t"
      "s_lshl_b32 s92, s92, 9\n\t"
      "s_lshl_b32 s93, s93, 10\n\t"
      "v_or3_b32 %[qw], %[qw], %[a2], s92\n\t"
      "v_or3_b32 %[kw], %[kw], %[a1], s93\n\t"
      "v_or_b32 %[a1], 0x8000, %[kw]\n\t"
      "v_or_b32 %[a2], 0x2000, %[kw]\n\t"
      "v_or_b32 %[a3], 0xa000, %[kw]\n\t"
      "s_waitcnt lgkmcnt(0)\n\t"
      "global_load_dwordx4 %[u0], %[kw], s[4:5] nt\n\t"
      "global_load_dwordx4 %[u1], %[a1], s[4:5] nt\n\t"
      "global_load_dwordx4 %[u2], %[a2], s[4:5] nt\n\t"
      "global_load_dwordx4 %[u3], %[a3], s[4:5] nt\n\t"
      "global_load_dwordx4 %[q], %[qw], s[6:7]\n\t"
      "s_memrealtime %[org]\n\t"
      "s_memrealtime s[90:91]"
      : [ka] "={s[4:19]}"(ka), [org] "={s[92:93]}"(org), [kw] "=&v"(kw), [qw] "=&v"(qw), [pf] "=&v"(pf), [a1] "=&v"(a1), [a2] "=&v"(a2),
        [a3] "=&v"(a3), [q] "=&v"(Q), [u0] "=&v"(U[0]), [u1] "=&v"(U[1]), [u2] "=&v"(U[2]), [u3] "=&v"(U[3])
      : [kp] "s"(__builtin_amdgcn_kernarg_segment_ptr()), [wg] "s"(wg), [tid] "v"(threadIdx.x)
      : "s90", "s91", "scc", "memory");
  static_assert(unit_off(1) == 0x8000 && unit_off(2) == 0x2000 && unit_off(3) == 0xa000, "the opening block's offsets");
  __builtin_amdgcn_sched_barrier(0);
#define KARG(i) ((void*)(((unsigned long long)ka[2 * (i) + 1] << 32) | ka[2 * (i)]))
  const unsigned lds0 = (unsigned)(uintptr_t)(LAS unsigned char*)lds;
  const int pair = wg / NSPLIT, split = wg % NSPLIT;
  const int lane = threadIdx.x & 63, wave = __builtin_amdgcn_readfirstlane(threadIdx.x >> 6);
  const int koff = kw;
  const int voff = (kw & ~0x3ff) | (lane * 16);

  const __amdgpu_buffer_rsrc_t rk = __builtin_amdgcn_make_buffer_rsrc(KARG(0), 0, NPAIR * CTX * D * 2, 0x00020000);
  const __amdgpu_buffer_rsrc_t rv = __builtin_amdgcn_make_buffer_rsrc(KARG(2), 0, NPAIR * CTX * D * 2, 0x00020000);
  const __amdgpu_buffer_rsrc_t rws = __builtin_amdgcn_make_buffer_rsrc(KARG(3), 0, WS_BYTES, 0x00020000);
  const __amdgpu_buffer_rsrc_t rkt = __builtin_amdgcn_make_buffer_rsrc(KARG(4), 0, NPAIR * D * 2, 0x00020000);
  const __amdgpu_buffer_rsrc_t rvt = __builtin_amdgcn_make_buffer_rsrc(KARG(5), 0, NPAIR * D * 2, 0x00020000);
  const __amdgpu_buffer_rsrc_t ro = __builtin_amdgcn_make_buffer_rsrc(KARG(6), 0, B_ * HQ * D * 2, 0x00020000);
  const __amdgpu_buffer_rsrc_t rm = __builtin_amdgcn_make_buffer_rsrc(KARG(7), 0, B_ * 2, 0x00020000);

  const bool treew = split == 0 && wave == 0;
  const int cons_split = CONS == -1 ? wave : CONS == -2 ? (wave & 3) : CONS == -3 ? (wave & 1) : CONS;
  const bool cons = split == cons_split;
  u32x4 KT, VT;
  unsigned epoch, maskw;
#define STEP(j)                                                                                                               \
  do {                                                                                                                        \
    if ((j) + WHI >= W0 && (j) + WHI < 2 * NU) {                                                                              \
      __builtin_amdgcn_sched_barrier(0);                                                                                      \
      if (ALIGN && (j) + WHI == NU)                                              \
        asm volatile("s_waitcnt vmcnt(%1)\nLp%=:\n\ts_memrealtime s[90:91]\n\ts_waitcnt lgkmcnt(0)\n\ts_sub_i32 s89, s90, s92\n\t" \
                     "s_cmpk_lt_i32 s89, %0\n\ts_cbranch_scc1 Lp%="                                                           \
                     :: "n"(due((j) + WHI) + MIDOFS), "n"(WHI - 1 + extra((j) + WHI, WHI)), "{s[92:93]}"(org)                 \
                     : "s89", "s90", "s91", "scc", "memory");                                                                 \
      else if ((j) + WHI >= 2 * NU - ENDSPIN)                                                                                      \
        asm volatile("s_waitcnt vmcnt(%1)\nLp%=:\n\ts_memrealtime s[90:91]\n\ts_waitcnt lgkmcnt(0)\n\ts_sub_i32 s90, s90, s92\n\t" \
                     "s_cmpk_lt_i32 s90, %0\n\ts_cbranch_scc1 Lp%="                                                           \
                     :: "n"(due((j) + WHI) + ENDOFS), "n"(WHI - 1 + extra((j) + WHI, WHI)), "{s[92:93]}"(org)                 \
                     : "s90", "s91", "scc", "memory");                                                                        \
      else                                                                                                                    \
        asm volatile("s_waitcnt vmcnt(%1) lgkmcnt(0)\n\ts_sub_i32 s90, s90, s92\n\ts_cmpk_lt_i32 s90, %0\n\ts_cbranch_scc0 Lq%=\n\t" \
                     "s_waitcnt vmcnt(%2)\nLq%=:\n\ts_memrealtime s[90:91]"                                                   \
                     :: "n"(due((j) + WHI)), "n"(WHI - 1 + extra((j) + WHI, WHI)), "n"(WLO - 1 + extra((j) + WHI, WLO)),      \
                        "{s[92:93]}"(org) : "s90", "s91", "scc", "memory");                                                   \
      __builtin_amdgcn_sched_barrier(0);                                                                                      \
      {                                                                                                                       \
        const int n_ = (j) + WHI;                                                                                             \
        U[n_] = __builtin_amdgcn_raw_buffer_load_b128(n_ < NU ? rk : rv, n_ < NU ? koff : voff, unit_off(n_ % NU), NT_);      \
      }                                                                                                                       \
      __builtin_amdgcn_sched_barrier(0);                                                                                      \
      if ((j) + WHI == SN) {                                                                                                  \
        KT = __builtin_amdgcn_raw_buffer_load_b128(rkt, treew ? (kw & 0xff) : OOB, pair * D * 2, 0);                          \
        VT = __builtin_amdgcn_raw_buffer_load_b128(rvt, treew ? (lane & 15) * 16 : OOB, pair * D * 2, 0);                     \
        maskw = __builtin_amdgcn_raw_buffer_load_b32(rm, treew ? 0 : OOB, 0, 0);                                              \
        epoch = __builtin_amdgcn_raw_buffer_load_b32(rws, 0, pair * HDR_BYTES, SC1);                                          \
        __builtin_amdgcn_sched_barrier(0);                                                                                    \
      }                                                                                                                       \
    }                                                                                                                         \
  } while (0)
#pragma unroll
  for (int j = W0 - WHI; j < 0; ++j) STEP(j);
  float S[NU];
  float m = -INFINITY, st;
  u32x4 M;
  const int recs0 = (pair * WAVES + wave) * (NSPLIT * REC_BYTES);
  const int recs = RECX ? REC_OFF + (recs0 & 0x1fff) + ((recs0 & ~0x1fff) << 1) + (RECX - 1) * 0x2000 : REC_OFF + recs0;
  unsigned tag, lds_w, lds_o, lds_mw, lds_mr;
  int rec_off, out_off, gm_off, ml_off, ep_off, tm_off, tlook;
  u32x2 Gt;
  {
    f32x4 dprev;
#pragma unroll
    for (int j = 0; j < NU; ++j) {
      STEP(j);
      if (j == 0) asm volatile("s_waitcnt vmcnt(%2)" : "+v"(Q), "+v"(pf) : "n"(WHI - W0 + 1 + (SN <= WHI ? 4 : 0)));
      if (j < W0) asm volatile("" : "+v"(U[j]));
      const f32x4 d = mfma16(U[j], Q, f32x4{0.f, 0.f, 0.f, 0.f});
      if (j > 0) {
        S[j - 1] = quad_diag_sum(dprev);
        if (j % 2 == 0) m = fmaxf(fmaxf(m, S[j - 2]), S[j - 1]);
        asm volatile("" : "+v"(S[j - 1]), "+v"(m));
      }
      dprev = d;
      int ln = lane;
      if ((j >= 4 && j <= 8 && j % 2 == 0) || j == TSTEP) asm volatile("" : "+v"(ln));
      if (j == 4) {
        const unsigned hm = (ln & 1) ? 0xffff0000u : 0x0000ffffu;
        const int w = (ln & 7) >> 1;
#pragma unroll
        for (int i = 0; i < 4; ++i) M[i] = (w == i) ? hm : 0u;
        asm volatile("" : "+v"(M));
      }
      if (j == 6) {
        lds_w = lds0 + wave * LDS_OSTR + (ln >> 5) * 512 + (ln & 15) * 32 + (ln & 16);
        lds_o = lds0 + (ln & 7) * LDS_OSTR + wave * 128 + (ln >> 3) * 16;
        lds_mw = lds0 + LDS_ML + wave * 16 + (ln & 8);
        lds_mr = lds0 + LDS_ML + (ln & 7) * 16 + (wave >> 2) * 8;
        asm volatile("" : "+v"(lds_w), "+v"(lds_o), "+v"(lds_mw), "+v"(lds_mr));
      }
      if (j == 8) {
        rec_off = cons ? ln * 16 : ((ln & 7) == split ? (ln & 7) * REC_BYTES + (ln >> 3) * 16 : OOB);
        out_off = (ln & 8) == 0 ? ((ln & 7) * 4 + (ln >> 4)) * 2 : OOB;
        gm_off = cons ? (ln & 7) * 16 : OOB;
        ml_off = (ln == 0 && (wave & 3) == 0) ? HDR_ML + (wave >> 2) * 128 + split * 16 : OOB;
        tm_off = (ln == 0 && wave == TWAVE) ? split * 8 : OOB;
        asm volatile("" : "+v"(tm_off));
        ep_off = (ln == 0 && wave == 0 && cons) ? 0 : OOB;
        asm volatile("" : "+v"(rec_off), "+v"(out_off), "+v"(gm_off), "+v"(ml_off), "+v"(ep_off) : "s"(ro));
      }
      if (j == TSTEP) {
        const float maskv = __uint_as_float(pair >= HKV ? (maskw & 0xffff0000u) : (maskw << 16));
        st = quad_diag_sum(mfma16(KT, Q, f32x4{0.f, 0.f, 0.f, 0.f})) + (1.0f - maskv) * (-1000000000.0f / SCALE);
        st = (treew && ln < 16) ? st : -INFINITY;
        m = fmaxf(m, st);
        tag = epoch * 1664525u + 1013904223u;
        asm volatile("" : "+v"(st), "+v"(m), "+v"(tag));
        __builtin_amdgcn_raw_buffer_store_b64(u32x2{(unsigned)org, tag}, rws, tm_off, pair * HDR_BYTES + HDR_TM, SC1);
      }
      if (j == ASTEP) Gt = __builtin_amdgcn_raw_buffer_load_b64(rws, (lane & 7) * 8, pair * HDR_BYTES + HDR_TM, SC1);
      if (j == ASTEP + WHI + 1) {
        const int dt = Gt[1] == tag ? (int)(Gt[0] - (unsigned)org) : 0;
        int ds = dt + __builtin_amdgcn_mov_dpp(dt, XOR1, 0xf, 0xf, true);
        ds = ds + __builtin_amdgcn_mov_dpp(ds, XOR2, 0xf, 0xf, true);
        ds = max(ds, __builtin_amdgcn_mov_dpp(ds, MIR8, 0xf, 0xf, true));
        int dm = max(dt, __builtin_amdgcn_mov_dpp(dt, XOR1, 0xf, 0xf, true));
        dm = max(dm, __builtin_amdgcn_mov_dpp(dm, XOR2, 0xf, 0xf, true));
        dm = max(dm, __builtin_amdgcn_mov_dpp(dm, MIR8, 0xf, 0xf, true));
        if (ALIGN) {
          const int sh = __builtin_amdgcn_readfirstlane(min(max((ALIGN == 1 ? (ds >> 2) : dm) + AOFS, -60), 300)) - (STAG * (7 - wave)) / 8;
          asm volatile("s_add_u32 s92, s92, %1" : "+{s[92:93]}"(org) : "s"(sh) : "scc");
          tlook = due(2 * NU - 1) + ENDOFS + TLOOK + (STAG * (7 - wave)) / 8;
        } else {
          tlook = __builtin_amdgcn_readfirstlane(min(dm, 300)) + due(2 * NU - 1) + ENDOFS + TLOOK;
        }
      }
      __builtin_amdgcn_sched_barrier(0);
    }
    S[NU - 1] = quad_diag_sum(dprev);
  }
  m = xg_max(fmaxf(fmaxf(m, S[NU - 2]), S[NU - 1]));
  const float mc = m * C2;
  const float pt = __builtin_amdgcn_exp2f(fmaf(st, C2, -mc));
  float l = pt;
  f32x4 O = {0.f, 0.f, 0.f, 0.f};
  {
    const unsigned pp = pack_bf16(pt, pt);
    O = mfma16(u32x4{pp & M[0], pp & M[1], pp & M[2], pp & M[3]}, VT, O);
  }
  const int hdr_ml = pair * HDR_BYTES + HDR_ML + (wave >> 2) * 128;
  float fw, cs;
  u32x4 Gm, tb;
  unsigned wr0, wr1;

  unsigned P[NU];
  u32x4 A[NU];
#define MAKE_P(j)                                                              \
  do {                                                                         \
    const float p = __builtin_amdgcn_exp2f(fmaf(S[j], C2, -mc));               \
    l += p;                                                                    \
    P[j] = pack_bf16(p, p);                                                    \
    asm volatile("" : "+v"(P[j]), "+v"(l));                                    \
  } while (0)
#define MAKE_A(j)                                                              \
  do {                                                                         \
    A[j] = u32x4{P[j] & M[0], P[j] & M[1], P[j] & M[2], P[j] & M[3]};          \
    asm volatile("" : "+v"(A[j]));                                             \
  } while (0)
  constexpr int AH = WHI + 1;
  MAKE_P(0); MAKE_A(0);
  __builtin_amdgcn_sched_barrier(0);
#pragma unroll
  for (int j = 0; j < NU; ++j) {
    STEP(NU + j);
    if (j == BSTEP) {
      __builtin_amdgcn_s_barrier();
      asm volatile("" ::: "memory");
      const f32x2 ml = *lds_at<f32x2>(lds_mr);
      float Mw, Lw;
      fw = merge_ml(ml[0], ml[1], Mw, Lw);
      __builtin_amdgcn_raw_buffer_store_b128(u32x4{__float_as_uint(Mw), tag, __float_as_uint(Lw), tag}, rws,
                                             ml_off, pair * HDR_BYTES, SC1);
      asm volatile("" : "+v"(fw));
      __builtin_amdgcn_sched_barrier(0);
    }
    if (j == GSTEP) {
      Gm = __builtin_amdgcn_raw_buffer_load_b128(rws, gm_off, hdr_ml, SC1);
      __builtin_amdgcn_sched_barrier(0);
    }
    if (WARM && j == WSTEP) {
      wr0 = __builtin_amdgcn_raw_buffer_load_b32(rws, (WARM & 1) ? cons_split * REC_BYTES : OOB, recs, 0);
      wr1 = __builtin_amdgcn_raw_buffer_load_b32(ro, ((WARM & 2) && cons) ? 0 : OOB, pair * (2 * D * 2) + wave * 64, 0);
      __builtin_amdgcn_sched_barrier(0);
    }
    if (WARM && j == WSTEP + WHI + 1) asm volatile("" :: "v"(wr0), "v"(wr1));
    asm volatile("" : "+v"(O));
    O = mfma16(A[j], U[NU + j], O);
    if (j < NU / 4) {
#pragma unroll
      for (int i = 1 + 4 * j; i < 5 + 4 * j && i < NU; ++i) MAKE_P(i);
    }
    if (j == NU / 4 - 1) {
      l = xg_sum(l);
      *lds_at<f32x2>(lds_mw) = f32x2{mc, l};
    }
    if (j < AH) { MAKE_A(2 * j + 1); MAKE_A(2 * j + 2); }
    else if (j + AH + 1 < NU) MAKE_A(j + AH + 1);
    if (j == BSTEP + 2) {
      tb = u32x4{tag & 0xff, (tag >> 8) & 0xff, (tag >> 16) & 0xff, tag >> 24};
      asm volatile("" : "+v"(tb));
    }
    if (j == GSTEP + WHI + 1) {
      asm volatile(
          "s_cmp_lg_u32 %[ca], %[cb]\n\t"
          "s_cbranch_scc1 Lh%=\n\t"
          "s_mov_b32 s87, 0x100000\n"
          "Lg%=:\n\t"
          "v_cmp_ne_u32 vcc, v125, %[tag]\n\t"
          "v_cmp_ne_u32 s[88:89], v127, %[tag]\n\t"
          "s_or_b64 vcc, vcc, s[88:89]\n\t"
          "s_cbranch_scc0 Lh%=\n\t"
          "buffer_load_dwordx4 v[124:127], %[off], %[rs], %[so] offen sc1\n\t"
          "s_sub_u32 s87, s87, 1\n\t"
          "s_waitcnt vmcnt(0)\n\t"
          "s_cbranch_scc0 Lg%=\n"
          "Lh%=:"
          : "+{v[124:127]}"(Gm)
          : [tag] "v"(tag), [ca] "s"(split), [cb] "s"(cons_split), [off] "v"(gm_off), [rs] "s"(rws), [so] "s"(hdr_ml)
          : "s87", "s88", "s89", "vcc", "scc", "memory");
      __builtin_amdgcn_raw_buffer_store_b32(tag, rws, ep_off, pair * HDR_BYTES, SC1);
      float MM, LL;
      cs = merge_ml(__uint_as_float(Gm[0]), __uint_as_float(Gm[2]), MM, LL) * __builtin_amdgcn_rcpf(LL);
      if (LAYB) cs = lane_pull((lane >> 3) * 4, cs);
      asm volatile("" : "+v"(cs));
    }
    __builtin_amdgcn_sched_barrier(0);
  }
  *lds_at<f32x4>(lds_w) = O;
  __syncthreads();
  f32x4 o = sum8(*lds_at<f32x4>(lds_o), fw);
  if (!cons) {
    u32x4 r;
#pragma unroll
    for (int i = 0; i < 4; ++i) r[i] = (__float_as_uint(o[i]) & ~0xffu) | tb[i];
    __builtin_amdgcn_raw_buffer_store_b128(r, rws, rec_off, recs, SC1);
  } else {
#pragma unroll
    for (int i = 0; i < 4; ++i) o[i] = lane_pull((lane & 7) * 32, o[i]);
    asm volatile(
        "s_lshl_b32 s84, %[sp], 3\n\t"
        "s_bfm_b64 s[84:85], 8, s84\n\t"
        "s_not_b64 exec, s[84:85]\n\t"
        "s_mov_b32 s87, 0x100000\n\t"
        "Lt%=:\n\t"
        "s_memrealtime s[90:91]\n\t"
        "s_waitcnt lgkmcnt(0)\n\t"
        "s_sub_i32 s90, s90, s92\n\t"
        "s_cmp_lt_i32 s90, %[tl]\n\t"
        "s_cbranch_scc1 Lt%=\n"
        "Lr%=:\n\t"
        "buffer_load_dwordx4 v[116:119], %[off], %[rs], %[so] offen sc1\n\t"
        "s_sub_u32 s87, s87, 1\n\t"
        "s_cbranch_scc1 Ls%=\n\t"
        "s_waitcnt vmcnt(0)\n\t"
        "v_xor_b32 v116, v116, v120\n\t"
        "v_xor_b32 v117, v117, v121\n\t"
        "v_xor_b32 v118, v118, v122\n\t"
        "v_xor_b32 v119, v119, v123\n\t"
        "v_or3_b32 v124, v116, v117, v118\n\t"
        "v_or_b32 v124, v124, v119\n\t"
        "v_and_b32 v124, 0xff, v124\n\t"
        "v_cmp_ne_u32 vcc, 0, v124\n\t"
        "s_and_b64 exec, vcc, vcc\n\t"
        "s_cbranch_scc1 Lr%=\n"
        "Ls%=:\n\t"
        "s_waitcnt vmcnt(0)\n\t"
        "s_mov_b64 exec, -1"
        : "+{v[116:119]}"(o)
        : "{v[120:123]}"(tb), [sp] "s"(split), [off] "v"(rec_off), [rs] "s"(rws), [so] "s"(recs), [sl] "n"(FDLY), [tl] "s"(tlook), "{s[92:93]}"(org)
        : "v124", "s84", "s85", "s87", "s90", "s91", "vcc", "scc", "memory");
    const float res = reduce_scatter4(o, cs);
    __builtin_amdgcn_raw_buffer_store_b16((unsigned short)pack_bf16(res, res), ro, out_off, pair * (2 * D * 2) + wave * 64, SC01);
  }
}

extern "C" void kernel_launch(void* const* d_in, const int* in_sizes, int n_in, void* d_out, int out_size, void* d_ws,
                              size_t ws_size, hipStream_t stream) {
  attn_tree_decode<<<NPAIR * NSPLIT, NTHR, 0, stream>>>((const unsigned short*)d_in[1], (const unsigned short*)d_in[0],
                                                        (const unsigned short*)d_in[2], (unsigned char*)d_ws,
                                                        (const unsigned short*)d_in[3], (const unsigned short*)d_in[4],
                                                        (unsigned short*)d_out, (const unsigned short*)d_in[5]);
}
```
